```python
import torch

def _softmax(x, axis=-1):
    return torch.softmax(x, dim=axis)
def _gelu(x):
    return torch.nn.functional.gelu(x, approximate="tanh")
def _silu(x):
    return torch.nn.functional.silu(x)
def _sigmoid(x):
    return torch.sigmoid(x)
def _layernorm(x, axis=-1, eps=1e-5):
    m = x.mean(dim=axis, keepdim=True)
    v = x.var(dim=axis, keepdim=True, unbiased=False)
    return (x - m) / torch.sqrt(v + eps)
def _scatter_add(tgt, idx, src, axis=0):
    return tgt.clone().index_add_(axis, idx, src)
def _segment_sum(src, seg, n):
    out = torch.zeros((n,) + src.shape[1:], dtype=src.dtype)
    return out.index_add_(0, seg, src)
def _pad(x, pw):
    flat = tuple(v for pair in reversed(pw) for v in pair)
    return torch.nn.functional.pad(x, flat)

kernel_name = 'standalone__glu_gated_geglu__sampling'

def setup_inputs():

    _w = (lambda a: (lambda t: t.to(torch.bfloat16) if t.is_floating_point() else t)(torch.as_tensor(a)))
    B, S, Dm, Df = 8, 4, 1024, 3584
    return {
        "x": _w((lambda *s: torch.randn(*s, dtype=torch.bfloat16) * 0.05)(B, S, Dm)),
        "w_gate": _w((lambda *s: torch.randn(*s, dtype=torch.bfloat16) * 0.05)(Dm, Df) * (Dm ** -0.5)),
        "w_up": _w((lambda *s: torch.randn(*s, dtype=torch.bfloat16) * 0.05)(Dm, Df) * (Dm ** -0.5)),
        "w_down": _w((lambda *s: torch.randn(*s, dtype=torch.bfloat16) * 0.05)(Df, Dm) * (Df ** -0.5)),
    }

def reference(x, w_gate, w_up, w_down):
    g = torch.einsum("bsd,df->bsf", x, w_gate)
    u = torch.einsum("bsd,df->bsf", x, w_up)
    h = _gelu(g) * u
    return torch.einsum("bsf,fd->bsd", h, w_down)
```

```cpp
#include <hip/hip_runtime.h>
#include <hip/hip_bf16.h>

#define GAS __attribute__((address_space(1)))
#define LAS __attribute__((address_space(3)))
typedef unsigned v4u __attribute__((ext_vector_type(4)));
typedef unsigned v2u __attribute__((ext_vector_type(2)));
typedef float f32x4 __attribute__((ext_vector_type(4)));
typedef short s16x8 __attribute__((ext_vector_type(8)));
typedef short s16x4 __attribute__((ext_vector_type(4)));
typedef __bf16 bf16x8 __attribute__((ext_vector_type(8)));

constexpr int M = 32, DM = 1024, DF = 3584;

__device__ __forceinline__ float bf2f(unsigned short b) { return __builtin_bit_cast(float, (unsigned)b << 16); }
__device__ __forceinline__ unsigned short f2bf(float f) { __bf16 b = (__bf16)f; return __builtin_bit_cast(unsigned short, b); }
__device__ __forceinline__ float rbf(float f) { return bf2f(f2bf(f)); }
__device__ __forceinline__ float gelu_tanh(float x) {
  const float y = 0.7978845608028654f * (x + 0.044715f * x * x * x);
  return x / (1.0f + __expf(-2.0f * y));
}
__device__ __forceinline__ f32x4 mfma16(v4u a, v4u b, f32x4 c) {
  return __builtin_amdgcn_mfma_f32_16x16x32_bf16(__builtin_bit_cast(bf16x8, a), __builtin_bit_cast(bf16x8, b), c, 0, 0, 0);
}
__device__ __forceinline__ v2u trread(LAS unsigned char* p) {
  s16x4 r = __builtin_amdgcn_ds_read_tr16_b64_v4i16((LAS s16x4*)p);
  return __builtin_bit_cast(v2u, r);
}
#define GLDS(src, dst, aux) __builtin_amdgcn_global_load_lds((const GAS unsigned*)(src), (LAS unsigned*)(dst), 16, 0, (aux))

__global__ void __launch_bounds__(512, 2) k_gateup(const unsigned short* __restrict__ x, const unsigned short* __restrict__ wg,
                                                   const unsigned short* __restrict__ wu, unsigned short* __restrict__ h) {
  __shared__ __attribute__((aligned(16))) unsigned char smem[65536];
  LAS unsigned char* L = (LAS unsigned char*)smem;
  const int tid = threadIdx.x, lane = tid & 63, w = __builtin_amdgcn_readfirstlane(tid >> 6);
  const int b = blockIdx.x, c = (b & 7) * 28 + (b >> 3);
  const int s = lane >> 1;
  const int prow = (s & 0x13) | ((s & 4) << 1) | ((s & 8) >> 1);
  const size_t goff = (size_t)(128 * w + prow) * DF + 16 * c + 8 * (lane & 1);
  LAS unsigned char* Lw = L + w * 8192;
#pragma unroll
  for (int i = 0; i < 4; ++i) {
    GLDS(wg + goff + (size_t)32 * i * DF, Lw + i * 1024, 0);
    GLDS(wu + goff + (size_t)32 * i * DF, Lw + 4096 + i * 1024, 0);
  }
  v4u xf[2][4];
#pragma unroll
  for (int i = 0; i < 4; ++i)
#pragma unroll
    for (int T = 0; T < 2; ++T)
      xf[T][i] = *(const GAS v4u*)(x + (size_t)(16 * T + (lane & 15)) * DM + 128 * w + 32 * i + 8 * (lane >> 4));
  asm volatile("s_waitcnt vmcnt(0)" ::: "memory");
  f32x4 ag[2] = {{0, 0, 0, 0}, {0, 0, 0, 0}}, au[2] = {{0, 0, 0, 0}, {0, 0, 0, 0}};
  LAS unsigned char* rp = Lw + (lane >> 5) * 512 + (lane & 31) * 8;
#pragma unroll
  for (int i = 0; i < 4; ++i) {
    v2u g0 = trread(rp + i * 1024), g1 = trread(rp + i * 1024 + 256);
    v2u u0 = trread(rp + 4096 + i * 1024), u1 = trread(rp + 4096 + i * 1024 + 256);
    v4u gf = {g0.x, g0.y, g1.x, g1.y}, uf = {u0.x, u0.y, u1.x, u1.y};
#pragma unroll
    for (int T = 0; T < 2; ++T) { ag[T] = mfma16(gf, xf[T][i], ag[T]); au[T] = mfma16(uf, xf[T][i], au[T]); }
  }
  LAS f32x4* red = (LAS f32x4*)Lw;
  red[0 * 64 + lane] = ag[0]; red[1 * 64 + lane] = ag[1]; red[2 * 64 + lane] = au[0]; red[3 * 64 + lane] = au[1];
  __syncthreads();
  if (tid < 128) {
    const int T = tid >> 6;
    f32x4 g = {0, 0, 0, 0}, u = {0, 0, 0, 0};
#pragma unroll
    for (int ww = 0; ww < 8; ++ww) {
      g += ((LAS f32x4*)(L + ww * 8192))[T * 64 + lane];
      u += ((LAS f32x4*)(L + ww * 8192))[(2 + T) * 64 + lane];
    }
    unsigned short hv[4];
#pragma unroll
    for (int j = 0; j < 4; ++j) hv[j] = f2bf(rbf(gelu_tanh(rbf(g[j]))) * rbf(u[j]));
    v2u o = {(unsigned)hv[0] | ((unsigned)hv[1] << 16), (unsigned)hv[2] | ((unsigned)hv[3] << 16)};
    *(GAS v2u*)(h + (size_t)(16 * T + (lane & 15)) * DF + 16 * c + 4 * (lane >> 4)) = o;
  }
}

__global__ void __launch_bounds__(256, 2) k_down(const unsigned short* __restrict__ h, const unsigned short* __restrict__ wd,
                                                 float* __restrict__ part) {
  __shared__ __attribute__((aligned(16))) unsigned char smem[28 * 1024];
  LAS unsigned char* L = (LAS unsigned char*)smem;
  const int tid = threadIdx.x, lane = tid & 63, w = __builtin_amdgcn_readfirstlane(tid >> 6);
  const int b = blockIdx.x, kq = b >> 4, nq = b & 15;
  const int s = lane >> 1;
  const int prow = (s & 0x13) | ((s & 4) << 1) | ((s & 8) >> 1);
  const size_t goff = (size_t)(224 * kq + prow) * DM + 64 * nq + 16 * w + 8 * (lane & 1);
  LAS unsigned char* Lw = L + w * 7168;
#pragma unroll
  for (int i = 0; i < 7; ++i) GLDS(wd + goff + (size_t)32 * i * DM, Lw + i * 1024, 0);
  v4u hf[2][7];
#pragma unroll
  for (int i = 0; i < 7; ++i)
#pragma unroll
    for (int T = 0; T < 2; ++T)
      hf[T][i] = *(const GAS v4u*)(h + (size_t)(16 * T + (lane & 15)) * DF + 224 * kq + 32 * i + 8 * (lane >> 4));
  asm volatile("s_waitcnt vmcnt(0)" ::: "memory");
  f32x4 acc[2] = {{0, 0, 0, 0}, {0, 0, 0, 0}};
  LAS unsigned char* rp = Lw + (lane >> 5) * 512 + (lane & 31) * 8;
#pragma unroll
  for (int i = 0; i < 7; ++i) {
    v2u d0 = trread(rp + i * 1024), d1 = trread(rp + i * 1024 + 256);
    v4u df = {d0.x, d0.y, d1.x, d1.y};
#pragma unroll
    for (int T = 0; T < 2; ++T) acc[T] = mfma16(df, hf[T][i], acc[T]);
  }
#pragma unroll
  for (int T = 0; T < 2; ++T)
    *(GAS f32x4*)(part + ((size_t)kq * 32 + 16 * T + (lane & 15)) * DM + 64 * nq + 16 * w + 4 * (lane >> 4)) = acc[T];
}

__global__ void __launch_bounds__(256) k_comb(const float* __restrict__ part, unsigned short* __restrict__ out) {
  const int i = blockIdx.x * 256 + threadIdx.x;
  f32x4 a = {0, 0, 0, 0};
#pragma unroll
  for (int q = 0; q < 16; ++q) a += *(const GAS f32x4*)(part + (size_t)q * 32768 + 4 * i);
  v2u o = {(unsigned)f2bf(a[0]) | ((unsigned)f2bf(a[1]) << 16), (unsigned)f2bf(a[2]) | ((unsigned)f2bf(a[3]) << 16)};
  *(GAS v2u*)(out + 4 * i) = o;
}

extern "C" void kernel_launch(void* const* d_in, const int* in_sizes, int n_in, void* d_out, int out_size, void* d_ws, size_t ws_size,
                              hipStream_t stream) {
  const unsigned short* x = (const unsigned short*)d_in[0];
  const unsigned short* wg = (const unsigned short*)d_in[1];
  const unsigned short* wu = (const unsigned short*)d_in[2];
  const unsigned short* wd = (const unsigned short*)d_in[3];
  unsigned short* h = (unsigned short*)d_ws;
  float* part = (float*)((char*)d_ws + 262144);
  k_gateup<<<224, 512, 0, stream>>>(x, wg, wu, h);
  k_down<<<256, 256, 0, stream>>>(h, wd, part);
  k_comb<<<32, 256, 0, stream>>>(part, (unsigned short*)d_out);
}
```

```cpp
#include <hip/hip_runtime.h>
#include <hip/hip_bf16.h>

#define GAS __attribute__((address_space(1)))
#define LAS __attribute__((address_space(3)))
typedef unsigned v4u __attribute__((ext_vector_type(4)));
typedef unsigned v2u __attribute__((ext_vector_type(2)));
typedef float f32x4 __attribute__((ext_vector_type(4)));
typedef short s16x4 __attribute__((ext_vector_type(4)));
typedef __bf16 bf16x8 __attribute__((ext_vector_type(8)));

constexpr int DM = 1024, DF = 3584;
constexpr int NWG = 224;
constexpr unsigned H_OFF = 4096;
constexpr unsigned P_OFF = H_OFF + NWG * 2048;
constexpr unsigned SPIN_CAP = 1u << 18;
#define WNT ""
#define STMP(i)

__device__ __forceinline__ float bf2f(unsigned short b) { return __builtin_bit_cast(float, (unsigned)b << 16); }
__device__ __forceinline__ unsigned short f2bf(float f) { __bf16 b = (__bf16)f; return __builtin_bit_cast(unsigned short, b); }
__device__ __forceinline__ unsigned f2u(float f) { return __builtin_bit_cast(unsigned, f); }
__device__ __forceinline__ float u2f(unsigned u) { return __builtin_bit_cast(float, u); }
__device__ __forceinline__ float rbf(float f) { return bf2f(f2bf(f)); }
__device__ __forceinline__ float gelu_tanh(float x) {
  const float y = 0.7978845608028654f * (x + 0.044715f * x * x * x);
  return x * __builtin_amdgcn_rcpf(1.0f + __expf(-2.0f * y));
}
__device__ __forceinline__ f32x4 mfma16(v4u a, v4u b, f32x4 c) {
  return __builtin_amdgcn_mfma_f32_16x16x32_bf16(__builtin_bit_cast(bf16x8, a), __builtin_bit_cast(bf16x8, b), c, 0, 0, 0);
}
__device__ __forceinline__ v2u trread(LAS unsigned char* p) {
  s16x4 r = __builtin_amdgcn_ds_read_tr16_b64_v4i16((LAS s16x4*)p);
  return __builtin_bit_cast(v2u, r);
}
__device__ __forceinline__ void dma16(const void* sbase, unsigned voff, unsigned lds) {
  asm volatile("s_mov_b32 m0, %2\n\ts_nop 0\n\tglobal_load_lds_dwordx4 %0, %1" WNT ::"v"(voff), "s"(sbase), "s"(lds) : "memory");
}
template <int OFF> __device__ __forceinline__ v4u ld16(const void* sbase, unsigned voff) {
  v4u r;
  asm volatile("global_load_dwordx4 %0, %1, %2 offset:%3" : "=v"(r) : "v"(voff), "s"(sbase), "i"(OFF) : "memory");
  return r;
}
template <int OFF> __device__ __forceinline__ v4u ld16_sc1(const void* sbase, unsigned voff) {
  v4u r;
  asm volatile("global_load_dwordx4 %0, %1, %2 offset:%3 sc1" : "=v"(r) : "v"(voff), "s"(sbase), "i"(OFF) : "memory");
  return r;
}
template <int OFF> __device__ __forceinline__ void st16_sc1(void* sbase, unsigned voff, v4u d) {
  asm volatile("global_store_dwordx4 %0, %1, %2 offset:%3 sc1\n\ts_nop 1" ::"v"(voff), "v"(d), "s"(sbase), "i"(OFF) : "memory");
}
#define WAITV(n) asm volatile("s_waitcnt vmcnt(" #n ")" ::: "memory")
#define BARRIER() do { asm volatile("s_waitcnt lgkmcnt(0)" ::: "memory"); __builtin_amdgcn_s_barrier(); asm volatile("" ::: "memory"); } while (0)

__global__ void __launch_bounds__(512, 2) geglu_fused(const unsigned char* __restrict__ x, const unsigned char* __restrict__ wg,
                                                      const unsigned char* __restrict__ wu, const unsigned char* __restrict__ wd,
                                                      unsigned short* __restrict__ out, unsigned char* __restrict__ ws) {
  __shared__ __attribute__((aligned(1024))) unsigned char smem[96 * 1024];
  LAS unsigned char* L = (LAS unsigned char*)smem;
  const unsigned lbase = (unsigned)(size_t)L;
  const int tid = threadIdx.x, lane = tid & 63, w = __builtin_amdgcn_readfirstlane(tid >> 6);
  const int b = blockIdx.x, c = (b & 7) * 28 + (b >> 3);
  const int kq = c >> 4, nq = c & 15;
  STMP(0);
  const int s = lane >> 1;
  const int prow = (s & 0x13) | ((s & 4) << 1) | ((s & 8) >> 1);
  const unsigned voffW = (unsigned)(128 * w + prow) * (DF * 2) + c * 32 + (lane & 1) * 16;
  const unsigned voffX = (unsigned)(lane & 15) * (DM * 2) + w * 256 + (lane >> 4) * 16;
  const unsigned lw = lbase + w * 8192;
  v4u xf[2][4];
  dma16(wg, voffW, lw);
  dma16(wu, voffW, lw + 1024);
  xf[0][0] = ld16<0>(x, voffX);
  xf[1][0] = ld16<0>(x + 32768, voffX);
  dma16(wg + 32 * DF * 2, voffW, lw + 2048);
  dma16(wu + 32 * DF * 2, voffW, lw + 3072);
  xf[0][1] = ld16<64>(x, voffX);
  xf[1][1] = ld16<64>(x + 32768, voffX);
  dma16(wg + 64 * DF * 2, voffW, lw + 4096);
  dma16(wu + 64 * DF * 2, voffW, lw + 5120);
  xf[0][2] = ld16<128>(x, voffX);
  xf[1][2] = ld16<128>(x + 32768, voffX);
  dma16(wg + 96 * DF * 2, voffW, lw + 6144);
  dma16(wu + 96 * DF * 2, voffW, lw + 7168);
  xf[0][3] = ld16<192>(x, voffX);
  xf[1][3] = ld16<192>(x + 32768, voffX);
  unsigned epoch;
  asm volatile("global_load_dword %0, %1, %2 sc1" : "=v"(epoch) : "v"(0u), "s"(ws) : "memory");
  {
    const int row = lane >> 3, ch = lane & 7;
    const unsigned voffD = (unsigned)(256 * kq + 32 * w + row) * (DM * 2) + nq * 128 + (((ch >> 1) ^ ((row >> 1) & 3)) * 32) + (ch & 1) * 16;
    const unsigned ld = lbase + 65536 + w * 4096;
    dma16(wd, voffD, ld);
    dma16(wd + 8 * DM * 2, voffD, ld + 1024);
    dma16(wd + 16 * DM * 2, voffD, ld + 2048);
    dma16(wd + 24 * DM * 2, voffD, ld + 3072);
  }
  STMP(1);
  f32x4 ag[2] = {{0, 0, 0, 0}, {0, 0, 0, 0}}, au[2] = {{0, 0, 0, 0}, {0, 0, 0, 0}};
  LAS unsigned char* rp = L + w * 8192 + (lane >> 5) * 512 + (lane & 31) * 8;
#define P1STEP(i, n)                                                                                        \
  {                                                                                                          \
    asm volatile("s_waitcnt vmcnt(" #n ")" : "+v"(xf[0][i]), "+v"(xf[1][i])::"memory");                      \
    v2u g0 = trread(rp + i * 2048), g1 = trread(rp + i * 2048 + 256);                                        \
    v2u u0 = trread(rp + i * 2048 + 1024), u1 = trread(rp + i * 2048 + 1024 + 256);                          \
    v4u gf = {g0.x, g0.y, g1.x, g1.y}, uf = {u0.x, u0.y, u1.x, u1.y};                                        \
    ag[0] = mfma16(gf, xf[0][i], ag[0]); au[0] = mfma16(uf, xf[0][i], au[0]);                                \
    ag[1] = mfma16(gf, xf[1][i], ag[1]); au[1] = mfma16(uf, xf[1][i], au[1]);                                \
  }
  P1STEP(0, 17)
  P1STEP(1, 13)
  P1STEP(2, 9)
  P1STEP(3, 5)
  asm volatile("s_waitcnt vmcnt(4)" : "+v"(epoch)::"memory");
  STMP(2);
  const unsigned enext = __builtin_amdgcn_readfirstlane(epoch) + 1u;
  const unsigned tag = enext * 0x9E3779B1u;
  {
    LAS f32x4* red = (LAS f32x4*)(L + w * 8192);
    red[0 * 64 + lane] = ag[0]; red[1 * 64 + lane] = ag[1]; red[2 * 64 + lane] = au[0]; red[3 * 64 + lane] = au[1];
  }
  BARRIER();
  if (w < 2) {
    f32x4 g = {0, 0, 0, 0}, u = {0, 0, 0, 0};
#pragma unroll
    for (int ww = 0; ww < 8; ++ww) {
      g += ((LAS f32x4*)(L + ww * 8192))[w * 64 + lane];
      u += ((LAS f32x4*)(L + ww * 8192))[(2 + w) * 64 + lane];
    }
    unsigned short hv[4];
#pragma unroll
    for (int j = 0; j < 4; ++j) hv[j] = f2bf(rbf(gelu_tanh(rbf(g[j]))) * rbf(u[j]));
    v4u o = {(unsigned)hv[0] | ((unsigned)hv[1] << 16), tag, (unsigned)hv[2] | ((unsigned)hv[3] << 16), tag};
    st16_sc1<0>(ws, H_OFF + c * 2048 + w * 1024 + lane * 16, o);
    WAITV(1);
  } else {
    WAITV(0);
  }
  STMP(3);
  const unsigned voffH = H_OFF + (16 * kq + 2 * w) * 2048 + lane * 16;
  v4u h00 = ld16_sc1<0>(ws, voffH), h01 = ld16_sc1<1024>(ws, voffH), h10 = ld16_sc1<2048>(ws, voffH), h11 = ld16_sc1<3072>(ws, voffH);
  BARRIER();
  const int ct = w & 3, T = w >> 2;
  v4u df[8];
  {
    const int g = lane >> 4, q = (lane & 15) >> 2, p = lane & 3;
    LAS unsigned char* dp = L + 65536 + (4 * g + q) * 128 + ((ct ^ ((2 * g + (q >> 1)) & 3)) * 32) + p * 8;
#pragma unroll
    for (int ks = 0; ks < 8; ++ks) {
      v2u d0 = trread(dp + ks * 4096), d1 = trread(dp + ks * 4096 + 2048);
      df[ks] = v4u{d0.x, d0.y, d1.x, d1.y};
    }
  }
  for (unsigned spins = 0;; ++spins) {
    asm volatile("s_waitcnt vmcnt(0)" : "+v"(h00), "+v"(h01), "+v"(h10), "+v"(h11)::"memory");
    const bool ok = (h00.y == tag) & (h00.w == tag) & (h01.y == tag) & (h01.w == tag) & (h10.y == tag) & (h10.w == tag) & (h11.y == tag) & (h11.w == tag);
    if (__all(ok) || spins > SPIN_CAP) break;
    h00 = ld16_sc1<0>(ws, voffH); h01 = ld16_sc1<1024>(ws, voffH); h10 = ld16_sc1<2048>(ws, voffH); h11 = ld16_sc1<3072>(ws, voffH);
  }
  STMP(4);
  ((LAS v4u*)L)[(w * 2 + 0) * 64 + lane] = v4u{h00.x, h00.z, h10.x, h10.z};
  ((LAS v4u*)L)[(w * 2 + 1) * 64 + lane] = v4u{h01.x, h01.z, h11.x, h11.z};
  BARRIER();
  f32x4 acc = {0, 0, 0, 0};
#pragma unroll
  for (int ks = 0; ks < 8; ++ks) acc = mfma16(((LAS v4u*)L)[(ks * 2 + T) * 64 + lane], df[ks], acc);
  {
    const int g = lane >> 4;
    const unsigned voffP = P_OFF + ((nq * 16 + 8 * T + 2 * g) * 14 + kq) * 1024 + (ct * 16 + (lane & 15)) * 16;
    st16_sc1<0>(ws, voffP, v4u{f2u(acc[0]), tag, f2u(acc[1]), tag});
    st16_sc1<0>(ws, voffP + 14 * 1024, v4u{f2u(acc[2]), tag, f2u(acc[3]), tag});
  }
  STMP(5);
  if (w == 0 || (w == 1 && c < 32)) {
    const int o = w == 0 ? c : 224 + c;
    const unsigned voffQ = P_OFF + o * 14 * 1024 + lane * 16;
    v4u pv[14];
    for (unsigned spins = 0;; ++spins) {
      pv[0] = ld16_sc1<0>(ws, voffQ); pv[1] = ld16_sc1<1024>(ws, voffQ); pv[2] = ld16_sc1<2048>(ws, voffQ); pv[3] = ld16_sc1<3072>(ws, voffQ);
      pv[4] = ld16_sc1<0>(ws + 4096, voffQ); pv[5] = ld16_sc1<1024>(ws + 4096, voffQ); pv[6] = ld16_sc1<2048>(ws + 4096, voffQ); pv[7] = ld16_sc1<3072>(ws + 4096, voffQ);
      pv[8] = ld16_sc1<0>(ws + 8192, voffQ); pv[9] = ld16_sc1<1024>(ws + 8192, voffQ); pv[10] = ld16_sc1<2048>(ws + 8192, voffQ); pv[11] = ld16_sc1<3072>(ws + 8192, voffQ);
      pv[12] = ld16_sc1<0>(ws + 12288, voffQ); pv[13] = ld16_sc1<1024>(ws + 12288, voffQ);
      asm volatile("s_waitcnt vmcnt(0)"
                   : "+v"(pv[0]), "+v"(pv[1]), "+v"(pv[2]), "+v"(pv[3]), "+v"(pv[4]), "+v"(pv[5]), "+v"(pv[6]), "+v"(pv[7]), "+v"(pv[8]),
                     "+v"(pv[9]), "+v"(pv[10]), "+v"(pv[11]), "+v"(pv[12]), "+v"(pv[13])::"memory");
      bool ok = true;
#pragma unroll
      for (int q = 0; q < 14; ++q) ok &= (pv[q].y == tag) & (pv[q].w == tag);
      if (__all(ok) || spins > SPIN_CAP) break;
    }
    float s0 = 0.f, s1 = 0.f;
#pragma unroll
    for (int q = 0; q < 14; ++q) { s0 += u2f(pv[q].x); s1 += u2f(pv[q].z); }
    const int onq = o >> 4, tp = o & 15;
    GAS unsigned short* op = (GAS unsigned short*)out + (size_t)(2 * tp) * DM + onq * 64 + lane;
    op[0] = f2bf(s0);
    op[DM] = f2bf(s1);
    if (o == 0 && lane == 0) __hip_atomic_store((GAS unsigned*)ws, enext, __ATOMIC_RELAXED, __HIP_MEMORY_SCOPE_AGENT);
    STMP(6);
  }
}

extern "C" void kernel_launch(void* const* d_in, const int* in_sizes, int n_in, void* d_out, int out_size, void* d_ws, size_t ws_size,
                              hipStream_t stream) {
  geglu_fused<<<NWG, 512, 0, stream>>>((const unsigned char*)d_in[0], (const unsigned char*)d_in[1], (const unsigned char*)d_in[2],
                                       (const unsigned char*)d_in[3], (unsigned short*)d_out, (unsigned char*)d_ws);
}
```

```cpp
#include <hip/hip_runtime.h>
#include <hip/hip_bf16.h>

#define GAS __attribute__((address_space(1)))
#define LAS __attribute__((address_space(3)))
typedef unsigned v4u __attribute__((ext_vector_type(4)));
typedef unsigned v2u __attribute__((ext_vector_type(2)));
typedef float f32x4 __attribute__((ext_vector_type(4)));
typedef short s16x4 __attribute__((ext_vector_type(4)));
typedef __bf16 bf16x8 __attribute__((ext_vector_type(8)));

#define HELP 1
#define DSPLIT 1
#define DLY1 14
#define DLY2 14
#define DLY3 14

constexpr int DM = 1024, DF = 3584;
constexpr int NWG = 224, NTHR = 576;
constexpr unsigned H_OFF = 4096;
constexpr unsigned G_OFF = H_OFF + NWG * 2048;
constexpr unsigned P_OFF = G_OFF + NWG * 8192;
constexpr unsigned SPIN_CAP = 1u << 18;
constexpr unsigned L_W = 0;
constexpr unsigned L_X = 65536;
constexpr unsigned L_D = 98304;
constexpr unsigned L_E = 131072;
constexpr unsigned L_H = 0;
constexpr unsigned L_BYTES = L_E + 2048;
#define WNT ""
#define STMP(i)
#define CNT(i, v)
#define STMPV(i, v)

__device__ __forceinline__ float bf2f(unsigned short b) { return __builtin_bit_cast(float, (unsigned)b << 16); }
__device__ __forceinline__ unsigned short f2bf(float f) { __bf16 b = (__bf16)f; return __builtin_bit_cast(unsigned short, b); }
__device__ __forceinline__ unsigned f2u(float f) { return __builtin_bit_cast(unsigned, f); }
__device__ __forceinline__ float u2f(unsigned u) { return __builtin_bit_cast(float, u); }
__device__ __forceinline__ float rbf(float f) { return bf2f(f2bf(f)); }
__device__ __forceinline__ float gelu_tanh(float x) {
  constexpr float k1 = -2.0f * 0.7978845608028654f * 1.4426950408889634f, k3 = k1 * 0.044715f;
  const float e = __builtin_amdgcn_exp2f(x * __builtin_fmaf(x * x, k3, k1));
  return x * __builtin_amdgcn_rcpf(1.0f + e);
}
__device__ __forceinline__ f32x4 mfma16(v4u a, v4u b, f32x4 c) {
  return __builtin_amdgcn_mfma_f32_16x16x32_bf16(__builtin_bit_cast(bf16x8, a), __builtin_bit_cast(bf16x8, b), c, 0, 0, 0);
}
__device__ __forceinline__ v2u trread(LAS unsigned char* p) {
  s16x4 r = __builtin_amdgcn_ds_read_tr16_b64_v4i16((LAS s16x4*)p);
  return __builtin_bit_cast(v2u, r);
}
__device__ __forceinline__ void dma16(const void* sbase, unsigned voff, unsigned lds) {
  asm volatile("s_mov_b32 m0, %2\n\ts_nop 0\n\tglobal_load_lds_dwordx4 %0, %1" WNT ::"v"(voff), "s"(sbase), "s"(lds) : "memory");
}
__device__ __forceinline__ void dma16x(const void* sbase, unsigned voff, unsigned lds) {
  asm volatile("s_mov_b32 m0, %2\n\ts_nop 0\n\tglobal_load_lds_dwordx4 %0, %1" ::"v"(voff), "s"(sbase), "s"(lds) : "memory");
}
template <int OFF> __device__ __forceinline__ v4u ld16_sc1(const void* sbase, unsigned voff) {
  v4u r;
  asm volatile("global_load_dwordx4 %0, %1, %2 offset:%3 sc1" : "=v"(r) : "v"(voff), "s"(sbase), "i"(OFF) : "memory");
  return r;
}
template <int OFF> __device__ __forceinline__ unsigned ld4_sc1(const void* sbase, unsigned voff) {
  unsigned r;
  asm volatile("global_load_dword %0, %1, %2 offset:%3 sc1" : "=v"(r) : "v"(voff), "s"(sbase), "i"(OFF) : "memory");
  return r;
}
__device__ __forceinline__ void st16_sc1(void* sbase, unsigned voff, v4u d) {
  asm volatile("global_store_dwordx4 %0, %1, %2 sc1\n\ts_nop 1" ::"v"(voff), "v"(d), "s"(sbase) : "memory");
}
__device__ __forceinline__ void st8_sc1(void* sbase, unsigned voff, v2u d) {
  asm volatile("global_store_dwordx2 %0, %1, %2 sc1" ::"v"(voff), "v"(d), "s"(sbase) : "memory");
}
#define WAITV(n) asm volatile("s_waitcnt vmcnt(" #n ")" ::: "memory")
#define BARRIER() do { asm volatile("s_waitcnt lgkmcnt(0)" ::: "memory"); __builtin_amdgcn_s_barrier(); asm volatile("" ::: "memory"); } while (0)
__device__ __forceinline__ v4u tag4(f32x4 v, unsigned tb) {
  return v4u{__builtin_amdgcn_perm(f2u(v[0]) + 0x80u, tb, 0x07060500u), __builtin_amdgcn_perm(f2u(v[1]) + 0x80u, tb, 0x07060501u),
             __builtin_amdgcn_perm(f2u(v[2]) + 0x80u, tb, 0x07060502u), __builtin_amdgcn_perm(f2u(v[3]) + 0x80u, tb, 0x07060503u)};
}

__global__ void __launch_bounds__(NTHR, 3) geglu_fused(const unsigned char* __restrict__ x, const unsigned char* __restrict__ wg,
                                                       const unsigned char* __restrict__ wu, const unsigned char* __restrict__ wd,
                                                       unsigned short* __restrict__ out, unsigned char* __restrict__ ws) {
  __shared__ __attribute__((aligned(1024))) unsigned char smem[L_BYTES];
  LAS unsigned char* L = (LAS unsigned char*)smem;
  const unsigned lbase = (unsigned)(size_t)L;
  const int tid = threadIdx.x, lane = tid & 63, w = __builtin_amdgcn_readfirstlane(tid >> 6);
  const int b = blockIdx.x, xg = b & 7, r = b >> 3, half = xg >> 2, j = xg & 3, m = j & 1, kh = j >> 1;
  const unsigned char* wm = m ? wu : wg;
  const int sl = lane >> 3, ch = lane & 7;
  if (r >= 28) {
    const int t = 28 * half + ((3 - (int)((size_t)wm >> 7) - 28 * half) & 7) + 8 * (r - 28);
    if (t < 28 * half + 28 && w < 8) {
      const unsigned vo = (unsigned)(512 * kh + 256 + 8 * w + sl) * (DF * 2) + t * 128 + ch * 16;
#pragma unroll
      for (int i = 0; i < 4; ++i) dma16(wm + i * 64 * DF * 2, vo, lbase + w * 1024 + i * 8192);
      WAITV(0);
    }
    return;
  }
  const int tile = 28 * half + r, c = 4 * tile + j;
  const int kq = (1 - half) * 7 + (r >> 2), nq = 4 * j + (r & 3);
  const unsigned swz = (unsigned)((((ch >> 1) ^ ((sl >> 1) & 3)) * 32) + (ch & 1) * 16);
  const unsigned voffW0 = (unsigned)(512 * kh + 8 * (sl >> 2) + (sl & 3)) * (DF * 2) + tile * 128 + swz;
  const unsigned voffX0 = (unsigned)(lane & 15) * (DM * 2) + kh * 1024 + (lane >> 4) * 16;
  const unsigned voffD0 = (unsigned)(256 * kq + sl) * (DM * 2) + nq * 128 + swz;
  if (w == 8) {
    const unsigned v0 = voffW0, v1 = voffW0 + 16 * DF * 2, v2 = voffW0 + 4 * DF * 2, v3 = voffW0 + 20 * DF * 2;
#define LOAD_W(k0, k1)                                                                                     \
    _Pragma("unroll 1") for (int ks = k0; ks < k1; ++ks) {                                                 \
      const unsigned char* p = wm + ks * (32 * DF * 2);                                                    \
      const unsigned l = lbase + L_W + ks * 4096;                                                          \
      dma16(p, v0, l); dma16(p, v1, l + 1024); dma16(p, v2, l + 2048); dma16(p, v3, l + 3072);             \
    }
#define LOAD_D(i0, i1)                                                                                     \
    _Pragma("unroll 1") for (int i = i0; i < i1; i += 2) {                                                 \
      dma16(wd + i * (8 * DM * 2), voffD0, lbase + L_D + i * 1024);                                        \
      dma16(wd + (i + 1) * (8 * DM * 2), voffD0, lbase + L_D + i * 1024 + 1024);                           \
    }
    LOAD_W(4, 8)
#pragma unroll 1
    for (int ks = 8; ks < 16; ++ks) { dma16x(x + ks * 64, voffX0, lbase + L_X + ks * 2048); dma16x(x + ks * 64 + 32768, voffX0, lbase + L_X + ks * 2048 + 1024); }
    LOAD_W(8, 12)
    WAITV(32);
    __builtin_amdgcn_s_barrier();
    LOAD_W(12, 16)
    LOAD_D(0, 16)
    WAITV(32);
    __builtin_amdgcn_s_barrier();
    WAITV(16);
    __builtin_amdgcn_s_barrier();
    __builtin_amdgcn_s_barrier();
    WAITV(0);
    __builtin_amdgcn_s_barrier();
    return;
  }
  STMP(0);
  {
    const unsigned voffW = voffW0 + (unsigned)(32 * (w >> 2) + 16 * (w & 1) + 4 * ((w >> 1) & 1)) * (DF * 2);
    dma16(wm, voffW, lbase + L_W + w * 1024);
    dma16(wm + 64 * DF * 2, voffW, lbase + L_W + w * 1024 + 8192);
    dma16x(x + w * 64, voffX0, lbase + L_X + w * 2048);
    dma16x(x + w * 64 + 32768, voffX0, lbase + L_X + w * 2048 + 1024);
  }
  unsigned epoch, dummy;
  asm volatile("global_load_dword %0, %1, %2 sc1" : "=v"(epoch) : "v"(0u), "s"(ws) : "memory");
  asm volatile("global_load_dword %0, %1, %2" : "=v"(dummy) : "v"(0u), "s"((w & 1) ? (const unsigned char*)out : (const unsigned char*)ws + (2u << 20)) : "memory");
  STMP(1);
  const int T = w >> 2, ct = w & 3;
  const int g = lane >> 4, q4 = (lane & 15) >> 2, p4 = lane & 3;
  const unsigned troff = (unsigned)((4 * g + q4) * 128 + ((ct ^ ((2 * g + (q4 >> 1)) & 3)) * 32) + p4 * 8);
  LAS unsigned char* wp = L + L_W + troff;
  LAS v4u* xp = (LAS v4u*)(L + L_X) + T * 64 + lane;
  f32x4 acc = {0, 0, 0, 0};
  v4u xf[8];
#define P1STAGE(s)                                                                                   \
  _Pragma("unroll") for (int ks = 4 * (s); ks < 4 * (s) + 4; ++ks) {                                 \
    v2u a0 = trread(wp + ks * 4096), a1 = trread(wp + ks * 4096 + 2048);                             \
    acc = mfma16(v4u{a0.x, a0.y, a1.x, a1.y}, xf[ks & 7], acc);                                      \
  }
  asm volatile("s_waitcnt vmcnt(0)" : "+v"(epoch), "+v"(dummy)::"memory");
  BARRIER();
#pragma unroll
  for (int ks = 0; ks < 8; ++ks) xf[ks] = xp[ks * 128];
  P1STAGE(0)
  P1STAGE(1)
  const unsigned enext = __builtin_amdgcn_readfirstlane(epoch) + 1u;
  const unsigned tag = enext * 0x9E3779B1u;
  const unsigned tb = (enext & 255u) | (((enext + 85u) & 255u) << 8) | (((enext + 170u) & 255u) << 16) | ((enext + 51u) << 24);
  const unsigned tl = (tb >> (8 * (lane & 3))) & 255u;
  BARRIER();
#pragma unroll
  for (int ks = 0; ks < 8; ++ks) xf[ks] = xp[(8 + ks) * 128];
  P1STAGE(2)
  BARRIER();
  STMP(2);
  P1STAGE(3)
  STMPV(7, acc);
  if (ct != j) st16_sc1(ws, G_OFF + (4 * tile + ct) * 8192 + j * 2048 + T * 1024 + lane * 16, tag4(acc, tb));
  else ((LAS f32x4*)(L + L_E))[T * 64 + lane] = acc;
  BARRIER();
  {
    const float own = ((LAS float*)(L + L_E))[w * 64 + lane];
    const unsigned voffG = G_OFF + c * 8192 + w * 256 + lane * 4;
    const unsigned o1 = voffG + (j ^ 1) * 2048, o2 = voffG + (j ^ 2) * 2048, o3 = voffG + (j ^ 3) * 2048;
    unsigned r1, r2, r3;
    if (DLY1) __builtin_amdgcn_s_sleep(DLY1);
    for (unsigned spins = 0;; ++spins) {
      r1 = ld4_sc1<0>(ws, o1); r2 = ld4_sc1<0>(ws, o2); r3 = ld4_sc1<0>(ws, o3);
      asm volatile("s_waitcnt vmcnt(0)" : "+v"(r1), "+v"(r2), "+v"(r3)::"memory");
      if (spins == 0) STMP(8);
      r1 ^= tl; r2 ^= tl; r3 ^= tl;
      if (!__any(((r1 | r2 | r3) & 255u) != 0) || spins > SPIN_CAP) { CNT(12, spins + 1); break; }
    }
    STMP(9);
    const float same = own + u2f(r2), other = u2f(r1) + u2f(r3);
    const float gs = m ? other : same, us = m ? same : other;
    const unsigned hv = f2bf(rbf(gelu_tanh(rbf(gs))) * rbf(us));
    const unsigned nb = (unsigned)__builtin_amdgcn_update_dpp(0, (int)hv, 0xB1, 0xf, 0xf, true);
    if (!(lane & 1)) st8_sc1(ws, H_OFF + c * 2048 + w * 256 + lane * 4, v2u{hv | (nb << 16), tag});
  }
  BARRIER();
  STMP(3);
  {
    const unsigned voffD = voffD0 + (unsigned)(8 * w) * (DM * 2);
    dma16(wd + 128 * DM * 2, voffD, lbase + L_D + w * 1024 + 16384); dma16(wd + 192 * DM * 2, voffD, lbase + L_D + w * 1024 + 24576);
  }
  const unsigned voffH = H_OFF + (16 * kq + 2 * w) * 2048 + lane * 16;
  LAS unsigned char* dp = L + L_D + troff;
  v4u df[8];
  constexpr int NPRE = DSPLIT == 0 ? 8 : DSPLIT == 1 ? 4 : 0;
#pragma unroll
  for (int ks = 0; ks < NPRE; ++ks) { v2u d0 = trread(dp + ks * 4096), d1 = trread(dp + ks * 4096 + 2048); df[ks] = v4u{d0.x, d0.y, d1.x, d1.y}; }
  if (DLY2) __builtin_amdgcn_s_sleep(DLY2);
  v4u h00, h01, h10, h11;
  for (unsigned spins = 0;; ++spins) {
    h00 = ld16_sc1<0>(ws, voffH); h01 = ld16_sc1<1024>(ws, voffH); h10 = ld16_sc1<2048>(ws, voffH); h11 = ld16_sc1<3072>(ws, voffH);
    asm volatile("s_waitcnt vmcnt(0)" : "+v"(h00), "+v"(h01), "+v"(h10), "+v"(h11)::"memory");
    if (spins == 0) STMP(10);
    const unsigned bad = (h00.y ^ tag) | (h00.w ^ tag) | (h01.y ^ tag) | (h01.w ^ tag) | (h10.y ^ tag) | (h10.w ^ tag) | (h11.y ^ tag) | (h11.w ^ tag);
    if (!__any(bad != 0) || spins > SPIN_CAP) { CNT(13, spins + 1); break; }
  }
  STMP(4);
  ((LAS v4u*)(L + L_H))[(w * 2 + 0) * 64 + lane] = v4u{h00.x, h00.z, h10.x, h10.z};
  ((LAS v4u*)(L + L_H))[(w * 2 + 1) * 64 + lane] = v4u{h01.x, h01.z, h11.x, h11.z};
  BARRIER();
  f32x4 a2 = {0, 0, 0, 0};
  v4u hf[8];
#pragma unroll
  for (int ks = 0; ks < 8; ++ks) hf[ks] = ((LAS v4u*)(L + L_H))[(ks * 2 + T) * 64 + lane];
#pragma unroll
  for (int ks = NPRE; ks < 8; ++ks) { v2u d0 = trread(dp + ks * 4096), d1 = trread(dp + ks * 4096 + 2048); df[ks] = v4u{d0.x, d0.y, d1.x, d1.y}; }
#pragma unroll
  for (int ks = 0; ks < 8; ++ks) a2 = mfma16(df[ks], hf[ks], a2);
  st16_sc1(ws, P_OFF + ((nq * 8 + w) * 14 + kq) * 1024 + lane * 16, tag4(a2, tb));
  STMP(5);
  if (w < 4 && r < 16) {
    const int onq = 4 * ((j + 1) & 3) + (r & 3), ot = 4 * half + (r >> 2);
    const unsigned voffP = P_OFF + (onq * 8 + ot) * 14 * 1024 + w * 256 + lane * 4;
    if (DLY3) __builtin_amdgcn_s_sleep(DLY3);
    unsigned pv[14];
    for (unsigned spins = 0;; ++spins) {
      pv[0] = ld4_sc1<0>(ws, voffP); pv[1] = ld4_sc1<1024>(ws, voffP); pv[2] = ld4_sc1<2048>(ws, voffP); pv[3] = ld4_sc1<3072>(ws, voffP);
      pv[4] = ld4_sc1<0>(ws + 4096, voffP); pv[5] = ld4_sc1<1024>(ws + 4096, voffP); pv[6] = ld4_sc1<2048>(ws + 4096, voffP); pv[7] = ld4_sc1<3072>(ws + 4096, voffP);
      pv[8] = ld4_sc1<0>(ws + 8192, voffP); pv[9] = ld4_sc1<1024>(ws + 8192, voffP); pv[10] = ld4_sc1<2048>(ws + 8192, voffP); pv[11] = ld4_sc1<3072>(ws + 8192, voffP);
      pv[12] = ld4_sc1<0>(ws + 12288, voffP); pv[13] = ld4_sc1<1024>(ws + 12288, voffP);
      asm volatile("s_waitcnt vmcnt(0)"
                   : "+v"(pv[0]), "+v"(pv[1]), "+v"(pv[2]), "+v"(pv[3]), "+v"(pv[4]), "+v"(pv[5]), "+v"(pv[6]), "+v"(pv[7]), "+v"(pv[8]),
                     "+v"(pv[9]), "+v"(pv[10]), "+v"(pv[11]), "+v"(pv[12]), "+v"(pv[13])::"memory");
      if (spins == 0) STMP(11);
      unsigned bad = 0;
#pragma unroll
      for (int k = 0; k < 14; ++k) { pv[k] ^= tl; bad |= pv[k]; }
      if (!__any((bad & 255u) != 0) || spins > SPIN_CAP) { CNT(14, spins + 1); break; }
    }
    float s0 = u2f(pv[0]), s1 = u2f(pv[1]);
#pragma unroll
    for (int k = 2; k < 14; k += 2) { s0 += u2f(pv[k]); s1 += u2f(pv[k + 1]); }
    ((GAS unsigned short*)out)[(size_t)(16 * (ot >> 2) + (lane >> 2)) * DM + onq * 64 + (ot & 3) * 16 + 4 * w + (lane & 3)] = f2bf(s0 + s1);
    if (b == 0 && tid == 0) __hip_atomic_store((GAS unsigned*)ws, enext, __ATOMIC_RELAXED, __HIP_MEMORY_SCOPE_AGENT);
    STMP(6);
  }
}

extern "C" void kernel_launch(void* const* d_in, const int* in_sizes, int n_in, void* d_out, int out_size, void* d_ws, size_t ws_size,
                              hipStream_t stream) {
  geglu_fused<<<HELP ? 256 : NWG, NTHR, 0, stream>>>((const unsigned char*)d_in[0], (const unsigned char*)d_in[1], (const unsigned char*)d_in[2],
                                                     (const unsigned char*)d_in[3], (unsigned short*)d_out, (unsigned char*)d_ws);
}
```

```cpp
#include <hip/hip_runtime.h>
#include <hip/hip_bf16.h>

#define LAS __attribute__((address_space(3)))
typedef unsigned v4u __attribute__((ext_vector_type(4)));
typedef unsigned v2u __attribute__((ext_vector_type(2)));
typedef float f32x4 __attribute__((ext_vector_type(4)));
typedef float f32x2 __attribute__((ext_vector_type(2)));
typedef __bf16 bf16x2 __attribute__((ext_vector_type(2)));
typedef __bf16 bf16x8 __attribute__((ext_vector_type(8)));
typedef short s16x4 __attribute__((ext_vector_type(4)));
typedef unsigned long long u64x4 __attribute__((ext_vector_type(4)));
typedef unsigned long long u64x2 __attribute__((ext_vector_type(2)));

constexpr int DM = 1024, DF = 3584, NTHR = 576;
constexpr int DLY1 = 10, DLY2 = 16;
constexpr unsigned TH = 200;
constexpr unsigned SPIN_CAP = 1u << 18;

constexpr unsigned S_WORD = 256, PROBE = (2u << 20) - 16384, G_OFF = 16384, P_OFF = 2u << 20;
constexpr unsigned L_W = 0;
constexpr unsigned L_X = 65536;
constexpr unsigned L_D = 98304;
constexpr unsigned L_E = 131072;
constexpr unsigned L_S = L_E + 2048;
constexpr unsigned L_H = 0;
constexpr unsigned L_BYTES = L_S + 256;

__device__ __forceinline__ unsigned f2u(float f) { return __builtin_bit_cast(unsigned, f); }
__device__ __forceinline__ float u2f(unsigned u) { return __builtin_bit_cast(float, u); }
__device__ __forceinline__ unsigned pk2(float a, float b) { return __builtin_bit_cast(unsigned, __builtin_convertvector(f32x2{a, b}, bf16x2)); }
__device__ __forceinline__ float rbf(float f) { return u2f((unsigned)__builtin_bit_cast(unsigned short, (__bf16)f) << 16); }
__device__ __forceinline__ float gelu_tanh(float x) {
  constexpr float k1 = -2.0f * 0.7978845608028654f * 1.4426950408889634f, k3 = k1 * 0.044715f;
  const float e = __builtin_amdgcn_exp2f(x * __builtin_fmaf(x * x, k3, k1));
  return x * __builtin_amdgcn_rcpf(1.0f + e);
}
__device__ __forceinline__ unsigned tag_of(unsigned enext) { return enext * 0x9E3779B1u + 0x7F4A7C15u; }
__device__ __forceinline__ f32x4 mfma16(v4u a, v4u b, f32x4 c) {
  return __builtin_amdgcn_mfma_f32_16x16x32_bf16(__builtin_bit_cast(bf16x8, a), __builtin_bit_cast(bf16x8, b), c, 0, 0, 0);
}
__device__ __forceinline__ v2u trread(LAS unsigned char* p) {
  return __builtin_bit_cast(v2u, __builtin_amdgcn_ds_read_tr16_b64_v4i16((LAS s16x4*)p));
}
__device__ __forceinline__ void dma16(const void* sbase, unsigned voff, unsigned lds) {
  asm volatile("s_mov_b32 m0, %2\n\ts_nop 0\n\tglobal_load_lds_dwordx4 %0, %1" ::"v"(voff), "s"(sbase), "s"(lds) : "memory");
}
__device__ __forceinline__ void dma64(const void* sbase, unsigned v0, unsigned v1, unsigned v2, unsigned v3, unsigned lds) {
  asm volatile("s_mov_b32 m0, %5\n\ts_nop 0\n\tglobal_load_lds_dwordx4 %0, %4\n\tglobal_load_lds_dwordx4 %1, %4 offset:1024\n\t"
               "global_load_lds_dwordx4 %2, %4 offset:2048\n\tglobal_load_lds_dwordx4 %3, %4 offset:3072" ::"v"(v0), "v"(v1), "v"(v2), "v"(v3), "s"(sbase), "s"(lds) : "memory");
}
template <int OFF> __device__ __forceinline__ v4u ld16_sc1(const void* sbase, unsigned voff) {
  v4u r;
  asm volatile("global_load_dwordx4 %0, %1, %2 offset:%3 sc1" : "=v"(r) : "v"(voff), "s"(sbase), "i"(OFF) : "memory");
  return r;
}
template <int OFF> __device__ __forceinline__ unsigned ld4_sc1(const void* sbase, unsigned voff) {
  unsigned r;
  asm volatile("global_load_dword %0, %1, %2 offset:%3 sc1" : "=v"(r) : "v"(voff), "s"(sbase), "i"(OFF) : "memory");
  return r;
}
__device__ __forceinline__ void st16_sc1(void* sbase, unsigned voff, v4u d) {
  asm volatile("global_store_dwordx4 %0, %1, %2 sc1\n\ts_nop 1" ::"v"(voff), "v"(d), "s"(sbase) : "memory");
}
__device__ __forceinline__ void st4_sc1(void* sbase, unsigned voff, unsigned d) {
  asm volatile("global_store_dword %0, %1, %2 sc1" ::"v"(voff), "v"(d), "s"(sbase) : "memory");
}
__device__ __forceinline__ unsigned bfi(unsigned mask, unsigned a, unsigned b) {
  unsigned r;
  asm("v_bfi_b32 %0, %1, %2, %3" : "=v"(r) : "v"(mask), "v"(a), "s"(b));
  return r;
}
#define WAITV(n) asm volatile("s_waitcnt vmcnt(%0)" ::"i"(n) : "memory")
#define BARRIER() do { asm volatile("s_waitcnt lgkmcnt(0)" ::: "memory"); __builtin_amdgcn_s_barrier(); asm volatile("" ::: "memory"); } while (0)
#define ALL_ZERO(v) (__builtin_amdgcn_ballot_w64((v) != 0) == 0)
#define PINV(x) asm volatile("" : "+v"(x))
#define PINS(x) asm volatile("" : "+s"(x))

__global__ void __launch_bounds__(NTHR, 3) geglu_fused(const unsigned char* x_, const unsigned char* wg_, const unsigned char* wu_,
                                                       const unsigned char* wd_, unsigned short* out_, unsigned char* ws_) {
  const auto* ka = __builtin_amdgcn_kernarg_segment_ptr();
  u64x4 a0;
  u64x2 a1;
  asm volatile("s_load_dwordx8 %0, %2, 0x0\n\ts_load_dwordx4 %1, %2, 0x20" : "=&s"(a0), "=&s"(a1) : "s"(ka));
  __builtin_amdgcn_sched_barrier(0);
  __shared__ __attribute__((aligned(1024))) unsigned char smem[L_BYTES];
  LAS unsigned char* L = (LAS unsigned char*)smem;
  const unsigned lbase = (unsigned)(size_t)L;
  const int tid = threadIdx.x, lane = tid & 63, w = __builtin_amdgcn_readfirstlane(tid >> 6);
  const int b = blockIdx.x, xg = b & 7, r = ((b >> 3) + 28) & 31, half = xg >> 2, j = xg & 3, m = j & 1, kh = j >> 1;
  const int sl = lane >> 3, ch = lane & 7;
  const int tile = 28 * half + r;
  const int kq = (1 - half) * 7 + (r >> 2), nq = 4 * j + (r & 3);
  const unsigned swz = (unsigned)((((ch >> 1) ^ ((sl >> 1) & 3)) * 32) + (ch & 1) * 16);
  const unsigned w0 = (unsigned)(512 * kh + 8 * (sl >> 2) + (sl & 3)) * (DF * 2) + tile * 128 + swz;
  const unsigned w1 = w0 + 16 * DF * 2 - 1024, w2 = w0 + 4 * DF * 2 - 2048, w3 = w0 + 20 * DF * 2 - 3072;
  const unsigned x0 = (unsigned)sl * (DM * 2) + kh * 1024 + (ch ^ (sl >> 1)) * 16;
  const unsigned x1 = (x0 ^ 64) + 16384 - 1024, x2 = x0 + 32768 - 2048, x3 = (x0 ^ 64) + 49152 - 3072;
  const bool fx = (w & 4) != 0;
  unsigned long long fb;
  asm volatile("s_load_dwordx2 %0, %1, %2" : "=&s"(fb) : "s"(ka), "s"(fx ? 0 : 8 + 8 * m));
  const unsigned fo = (w & 3) * (fx ? 128 : 32 * DF * 2), fl = lbase + (fx ? L_X : L_W) + (w & 3) * 4096;
  asm volatile("s_mov_b32 m0, %7\n\ts_cmp_lg_u32 %8, 0\n\ts_waitcnt lgkmcnt(0)\n\ts_cbranch_scc1 1f\n\t"
               "global_load_lds_dwordx4 %3, %0\n\tglobal_load_lds_dwordx4 %4, %0 offset:1024\n\t"
               "global_load_lds_dwordx4 %5, %0 offset:2048\n\tglobal_load_lds_dwordx4 %6, %0 offset:3072\n1:"
               : "+s"(fb), "+s"(a0), "+s"(a1)
               : "v"((fx ? x0 : w0) + fo), "v"((fx ? x1 : w1) + fo), "v"((fx ? x2 : w2) + fo), "v"((fx ? x3 : w3) + fo), "s"(fl), "s"(((r + 4) >> 5) | (w >> 3))
               : "memory", "scc");
  const unsigned char* x = (const unsigned char*)a0.x;
  const unsigned char* wm = (const unsigned char*)(m ? a0.z : a0.y);
  const unsigned char* wd = (const unsigned char*)a0.w;
  unsigned short* out = (unsigned short*)a1.x;
  unsigned char* ws = (unsigned char*)((a1.y + 0x1FFFFFull) & ~0x1FFFFFull);
  unsigned epoch = 0;
  if (__builtin_expect(r < 28 && w < 8, 1)) asm volatile("global_load_dword %0, %1, %2 sc1" : "+v"(epoch) : "v"(0u), "s"(ws) : "memory");

  if (__builtin_expect(w == 8, 1)) {
    if (r >= 28) __builtin_amdgcn_endpgm();
#define LOAD_W(k0, k1) _Pragma("unroll 1") for (int ks = k0; ks < k1; ++ks) dma64(wm + ks * (32 * DF * 2), w0, w1, w2, w3, lbase + L_W + ks * 4096)
    LOAD_W(4, 8);
#pragma unroll 1
    for (int q = 4; q < 8; ++q) dma64(x + q * 128, x0, x1, x2, x3, lbase + L_X + q * 4096);
    LOAD_W(8, 12);
    WAITV(32);
    __builtin_amdgcn_s_barrier();
    LOAD_W(12, 16);
    asm volatile("s_mov_b32 m0, %2\n\ts_nop 0\n\tglobal_load_lds_dword %0, %1 sc1" ::"v"(S_WORD), "s"(ws), "s"(lbase + L_S) : "memory");
    WAITV(17);
    __builtin_amdgcn_s_barrier();
    WAITV(0);
    __builtin_amdgcn_s_barrier();
    __builtin_amdgcn_endpgm();
  }
  if (__builtin_expect(r >= 28, 1)) {
    const int t = 28 * half + ((3 - (int)((size_t)wm >> 7) - 28 * half) & 7) + 8 * (r - 28);
    const unsigned th = (unsigned)__builtin_amdgcn_s_memrealtime() + TH;
    if (t < 28 * half + 28) {
      const unsigned vo = (unsigned)(512 * kh + 256 + 8 * w + sl) * (DF * 2) + t * 128 + ch * 16;
#pragma unroll
      for (int i = 0; i < 4; ++i) dma16(wm + i * 64 * DF * 2, vo, lbase + w * 1024 + i * 8192);
    } else if (j == 0 && w == 0) {
      unsigned ep;
      unsigned long long c0, c1;
      const unsigned pa = PROBE + lane * 4;
      asm volatile("global_store_dword %3, %5, %6 sc1\n\tglobal_store_dword %4, %5, %6 sc1\n\tglobal_load_dword %2, %5, %6 sc1\n\t"
                   "s_waitcnt vmcnt(2)\n\ts_memtime %0\n\ts_waitcnt vmcnt(1)\n\ts_memtime %1\n\ts_waitcnt vmcnt(0) lgkmcnt(0)"
                   : "=&s"(c0), "=&s"(c1), "=&v"(ep) : "v"(pa), "v"(pa + 8192u), "v"(0u), "s"(ws) : "memory");
      const unsigned par = (unsigned)half ^ ((unsigned)(c1 - c0) < 96u ? 1u : 0u);
      st4_sc1(ws, S_WORD + lane * 4, tag_of(ep + 1u) ^ (par << 21));
    }
    while ((int)((unsigned)__builtin_amdgcn_s_memrealtime() - th) < 0) __builtin_amdgcn_s_sleep(2);
    const unsigned vd = (unsigned)(1792 * (1 - half) + 128 + 16 * (r - 28) + 2 * w + (lane >> 5)) * (DM * 2) + j * 512 + (lane & 31) * 16;
#pragma unroll 1
    for (int q = 0; q < 2; ++q)
#pragma unroll 1
      for (int i = 0; i < 7; ++i) dma16(wd + (i * 256 + q * 64) * (DM * 2), vd, lbase + 32768 + w * 1024);
    WAITV(0);
    __builtin_amdgcn_endpgm();
  }

  const int T = w >> 2, ct = w & 3;
  const int g = lane >> 4, q4 = (lane & 15) >> 2, p4 = lane & 3;
  const bool owner = T == 0 && r < 16;
  const int onq = 4 * ((j + 1) & 3) + (r & 3), oct = (r >> 2) & 3;
  unsigned troff = (unsigned)((4 * g + q4) * 128 + ((ct ^ ((2 * g + (q4 >> 1)) & 3)) * 32) + p4 * 8);
  unsigned xo = L_X + T * 2048 + (lane & 15) * 128 + ((g ^ ((lane & 15) >> 1)) * 16), xo2 = xo ^ 64;
  unsigned hmask = 0xFFFFFF00u;
  const unsigned gblk = G_OFF + r * 65536 + half * 8192;
  unsigned oG = gblk + ct * 16384 + j * 2048 + T * 1024 + lane * 16;
  const unsigned voffG = gblk + j * 16384 + w * 256 + lane * 4;
  unsigned o1 = voffG + (j ^ 1) * 2048, o2 = voffG + (j ^ 2) * 2048, o3 = voffG + (j ^ 3) * 2048;
  unsigned oH = (voffG ^ 8192) + j * 2048;
  unsigned voffH = G_OFF + ((r & ~3) + (w >> 1)) * 65536 + half * 8192 + (w & 1) * (2 * 18432) + lane * 16;
  unsigned oP = P_OFF + ((nq * 4 + ct) * 2 + (kq >> 3)) * 16384 + (T ^ 1) * 8192 + (kq & 7) * 1024 + lane * 16;
  unsigned voffP = P_OFF + (onq * 4 + oct) * 32768 + (half ^ 1) * 8192 + ct * 256 + lane * 4;
  unsigned oF = P_OFF + (nq * 8 + 1) * 16384 + (lane >> 5) * 8192 + 6144 + kq * 128 + (lane & 31) * 4;
  unsigned voffF = P_OFF + (onq * 8 + 1) * 16384 + (half ^ 1) * 8192 + 6144 + min(lane & 15, 13) * 128;
  unsigned oO = (unsigned)((16 * half + (lane >> 2)) * DM + onq * 64 + oct * 16 + 4 * ct + (lane & 3)) * 2;
  unsigned lE = L_E + T * 1024 + lane * 16, lE2 = L_E + w * 256 + lane * 4, lHw = L_H + w * 2048 + lane * 16, lHr = L_H + T * 1024 + lane * 16;
  unsigned voffD = (unsigned)(256 * kq + 8 * w + sl) * (DM * 2) + nq * 128 + swz;
  PINV(troff); PINV(xo); PINV(xo2); PINV(hmask); PINV(oG); PINV(o1); PINV(o2); PINV(o3); PINV(oH); PINV(voffH); PINV(oP); PINV(voffP);
  PINV(oF); PINV(voffF); PINV(oO); PINV(lE); PINV(lE2); PINV(lHw); PINV(lHr); PINV(voffD);
#define XF(ks) *(LAS v4u*)(L + (((ks) & 1) ? xo2 : xo) + ((ks) >> 1) * 4096)
#define P1STAGE(s)                                                                                                     \
  {                                                                                                                    \
    v2u a[8];                                                                                                          \
    _Pragma("unroll") for (int i = 0; i < 8; ++i) a[i] = trread(L + L_W + troff + (s) * 16384 + i * 2048);             \
    __builtin_amdgcn_sched_barrier(0);                                                                                 \
    _Pragma("unroll") for (int i = 0; i < 4; ++i)                                                                      \
      acc = mfma16(v4u{a[2 * i].x, a[2 * i].y, a[2 * i + 1].x, a[2 * i + 1].y}, xf[(4 * (s) + i) & 7], acc);           \
    PINV(acc);                                                                                                         \
  }
#define LOAD_D(i0, i1) _Pragma("unroll") for (int i = i0; i < i1; ++i) dma16(wd + i * (64 * DM * 2), voffD, lbase + L_D + w * 1024 + i * 8192)
#define TAG4(v) v4u{bfi(hmask, f2u(v[0]), t0), bfi(hmask, f2u(v[1]), t1), bfi(hmask, f2u(v[2]), t2), bfi(hmask, f2u(v[3]), t3)}

  f32x4 acc = {0, 0, 0, 0};
  v4u xf[8];
  asm volatile("s_waitcnt vmcnt(0)" : "+v"(epoch)::"memory");
  BARRIER();
#pragma unroll
  for (int ks = 0; ks < 8; ++ks) xf[ks] = XF(ks);
  P1STAGE(0)
  P1STAGE(1)
  PINV(epoch);
  unsigned enext = __builtin_amdgcn_readfirstlane(epoch) + 1u;
  unsigned tag = tag_of(enext), tagv = tag;
  unsigned t0 = enext & 255u, t1 = (enext + (enext >> 8) + 85u) & 255u, t2 = (enext + (enext >> 16) + 170u) & 255u, t3 = (enext + (enext >> 24) + 51u) & 255u;
  unsigned tl = ((t0 | (t1 << 8) | (t2 << 16) | (t3 << 24)) >> (8 * (lane & 3))) & 255u;
  PINS(enext); PINS(tag); PINS(t0); PINS(t1); PINS(t2); PINS(t3); PINV(tl); PINV(tagv);
  BARRIER();
#pragma unroll
  for (int ks = 0; ks < 8; ++ks) xf[ks] = XF(8 + ks);
  P1STAGE(2)
  LOAD_D(0, 2);
  BARRIER();
  unsigned sw = *(LAS unsigned*)(L + L_S);
  P1STAGE(3)
  sw = __builtin_amdgcn_readfirstlane(sw) ^ tag;
  if (__builtin_expect((sw & ~(1u << 21)) != 0, 0)) {
    for (unsigned left = SPIN_CAP; left; --left) {
      unsigned v;
      asm volatile("global_load_dword %0, %1, %2 sc1\n\ts_waitcnt vmcnt(0)" : "=&v"(v) : "v"(S_WORD), "s"(ws) : "memory");
      sw = __builtin_amdgcn_readfirstlane(v) ^ tag;
      if ((sw & ~(1u << 21)) == 0) break;
    }
    sw &= 1u << 21;
  }
  unsigned char* hb = ws + sw;
  PINS(hb);
  asm volatile("s_nop 7" : "+v"(acc));

  if (ct != j) st16_sc1(hb, oG, TAG4(acc));
  else *(LAS f32x4*)(L + lE) = acc;
  BARRIER();
  {
    float own = *(LAS float*)(L + lE2);
    PINV(own);
    __builtin_amdgcn_s_sleep(DLY1);
    unsigned r1, r2, r3, bad, left = SPIN_CAP;
    do {
      r1 = ld4_sc1<0>(hb, o1); r2 = ld4_sc1<0>(hb, o2); r3 = ld4_sc1<0>(hb, o3);
      unsigned live = --left ? 255u : 0u;
      PINS(live);
      asm volatile("s_waitcnt vmcnt(0)" : "+v"(r1), "+v"(r2), "+v"(r3)::"memory");
      r1 ^= tl; r2 ^= tl; r3 ^= tl;
      bad = (r1 | r2 | r3) & live;
      PINV(bad);
    } while (!ALL_ZERO(bad));
    const float same = own + u2f(r2), other = u2f(r1) + u2f(r3);
    const float gs = m ? other : same, us = m ? same : other;
    const float hv = rbf(gelu_tanh(rbf(gs))) * rbf(us);
    const float nb = u2f((unsigned)__builtin_amdgcn_update_dpp(0, (int)f2u(hv), 0xB1, 0xf, 0xf, true));
    asm volatile("s_mov_b64 exec, %3\n\tglobal_store_dwordx2 %0, %1, %2 sc1\n\ts_mov_b64 exec, -1" ::"v"(oH), "v"(v2u{pk2(hv, nb), tagv}), "s"(hb), "s"(0x5555555555555555ull) : "memory");
  }
  unsigned fpre = 0;
  if (w == 0 && r < 16) asm volatile("global_load_dword %0, %1, %2 sc1" : "+v"(fpre) : "v"(voffF), "s"(hb) : "memory");
  LOAD_D(2, 4);

  __builtin_amdgcn_s_sleep(DLY2);
  v4u h00, h01, h10, h11;
#define HLOAD() do { h00 = ld16_sc1<0>(hb, voffH); h01 = ld16_sc1<1024>(hb, voffH); h10 = ld16_sc1<0>(hb + 18432, voffH); h11 = ld16_sc1<1024>(hb + 18432, voffH); } while (0)
#define HWAIT() asm volatile("s_waitcnt vmcnt(0)" : "+v"(h00), "+v"(h01), "+v"(h10), "+v"(h11)::"memory")
#define HBAD() (((h00.y ^ tag) | (h00.w ^ tag)) | ((h01.y ^ tag) | (h01.w ^ tag)) | ((h10.y ^ tag) | (h10.w ^ tag)) | ((h11.y ^ tag) | (h11.w ^ tag)))
#define HSTAGE() do { *(LAS v4u*)(L + lHw) = v4u{h00.x, h00.z, h10.x, h10.z}; *(LAS v4u*)(L + lHw + 1024) = v4u{h01.x, h01.z, h11.x, h11.z}; asm volatile("" ::: "memory"); } while (0)
  HLOAD();
  WAITV(4);
  BARRIER();
  v4u df[8];
  {
    unsigned dro = L_D + troff;
    PINV(dro);
#pragma unroll
    for (int ks = 0; ks < 8; ++ks) { v2u d0 = trread(L + dro + ks * 4096), d1 = trread(L + dro + ks * 4096 + 2048); df[ks] = v4u{d0.x, d0.y, d1.x, d1.y}; }
  }
  {
    HWAIT();
    HSTAGE();
    unsigned bad = HBAD();
    PINV(bad);
    if (__builtin_expect(!ALL_ZERO(bad), 1)) {
      unsigned left = SPIN_CAP;
      do {
        HLOAD();
        unsigned live = --left ? ~0u : 0u;
        PINV(live);
        HWAIT();
        HSTAGE();
        bad = HBAD() & live;
        PINV(bad);
      } while (!ALL_ZERO(bad));
    }
  }
  PINV(fpre);
  BARRIER();
  if (w == 7) st4_sc1(hb, oF, tagv);

  f32x4 a2 = {0, 0, 0, 0};
  v4u hf[8];
#pragma unroll
  for (int ks = 0; ks < 8; ++ks) hf[ks] = *(LAS v4u*)(L + lHr + ks * 2048);
  __builtin_amdgcn_sched_barrier(0);
#pragma unroll
  for (int ks = 0; ks < 8; ++ks) a2 = mfma16(df[ks], hf[ks], a2);
  unsigned f0 = 0;
  if (owner) asm volatile("global_load_dword %0, %1, %2 sc1" : "+v"(f0) : "v"(voffF), "s"(hb) : "memory");
  asm volatile("s_nop 7" : "+v"(a2));
  st16_sc1(hb, oP, TAG4(a2));

  if (owner) {
    asm volatile("s_waitcnt vmcnt(1)" : "+v"(f0)::"memory");
    if (!ALL_ZERO(f0 ^ tag)) {
      unsigned f, left = SPIN_CAP;
      do {
        f = ld4_sc1<0>(hb, voffF);
        unsigned live = --left ? ~0u : 0u;
        PINV(live);
        asm volatile("s_waitcnt vmcnt(0)" : "+v"(f)::"memory");
        f = (f ^ tag) & live;
        PINV(f);
      } while (!ALL_ZERO(f));
    }
    unsigned pv[14], bad, res, left = SPIN_CAP;
    do {
      pv[0] = ld4_sc1<0>(hb, voffP); pv[1] = ld4_sc1<1024>(hb, voffP); pv[2] = ld4_sc1<2048>(hb, voffP); pv[3] = ld4_sc1<3072>(hb, voffP);
      pv[4] = ld4_sc1<0>(hb + 4096, voffP); pv[5] = ld4_sc1<1024>(hb + 4096, voffP); pv[6] = ld4_sc1<2048>(hb + 4096, voffP);
      pv[7] = ld4_sc1<3072>(hb + 4096, voffP); pv[8] = ld4_sc1<0>(hb + 16384, voffP); pv[9] = ld4_sc1<1024>(hb + 16384, voffP);
      pv[10] = ld4_sc1<2048>(hb + 16384, voffP); pv[11] = ld4_sc1<3072>(hb + 16384, voffP); pv[12] = ld4_sc1<0>(hb + 20480, voffP);
      pv[13] = ld4_sc1<1024>(hb + 20480, voffP);
      unsigned live = --left ? 255u : 0u;
      PINS(live);
      asm volatile("s_waitcnt vmcnt(0)"
                   : "+v"(pv[0]), "+v"(pv[1]), "+v"(pv[2]), "+v"(pv[3]), "+v"(pv[4]), "+v"(pv[5]), "+v"(pv[6]), "+v"(pv[7]), "+v"(pv[8]),
                     "+v"(pv[9]), "+v"(pv[10]), "+v"(pv[11]), "+v"(pv[12]), "+v"(pv[13])::"memory");
      bad = 0;
#pragma unroll
      for (int k = 0; k < 14; ++k) { pv[k] ^= tl; bad |= pv[k]; }
      bad &= live;
      const f32x2 p0 = f32x2{u2f(pv[0]), u2f(pv[1])} + f32x2{u2f(pv[2]), u2f(pv[3])}, p1 = f32x2{u2f(pv[4]), u2f(pv[5])} + f32x2{u2f(pv[6]), u2f(pv[7])};
      const f32x2 p2 = f32x2{u2f(pv[8]), u2f(pv[9])} + f32x2{u2f(pv[10]), u2f(pv[11])};
      const f32x2 p3 = (p0 + p1) + (p2 + f32x2{u2f(pv[12]), u2f(pv[13])});
      res = pk2(p3.x + p3.y, 0.f);
      asm volatile("" : "+v"(bad), "+v"(res));
    } while (!ALL_ZERO(bad));
    PINV(f0);
    asm volatile("global_store_short %0, %1, %2" ::"v"(oO), "v"(res), "s"(out) : "memory");
    __builtin_amdgcn_sched_barrier(0);
    if (r == 0 && xg == 0 && w == 0) st4_sc1(ws, lane * 4, enext);
  }
}

extern "C" void kernel_launch(void* const* d_in, const int* in_sizes, int n_in, void* d_out, int out_size, void* d_ws, size_t ws_size,
                              hipStream_t stream) {
  geglu_fused<<<256, NTHR, 0, stream>>>((const unsigned char*)d_in[0], (const unsigned char*)d_in[1], (const unsigned char*)d_in[2],
                                        (const unsigned char*)d_in[3], (unsigned short*)d_out, (unsigned char*)d_ws);
}
```
